```python
import torch

def _softmax(x, axis=-1):
    return torch.softmax(x, dim=axis)
def _gelu(x):
    return torch.nn.functional.gelu(x, approximate="tanh")
def _silu(x):
    return torch.nn.functional.silu(x)
def _sigmoid(x):
    return torch.sigmoid(x)
def _layernorm(x, axis=-1, eps=1e-5):
    m = x.mean(dim=axis, keepdim=True)
    v = x.var(dim=axis, keepdim=True, unbiased=False)
    return (x - m) / torch.sqrt(v + eps)
def _scatter_add(tgt, idx, src, axis=0):
    return tgt.clone().index_add_(axis, idx, src)
def _segment_sum(src, seg, n):
    out = torch.zeros((n,) + src.shape[1:], dtype=src.dtype)
    return out.index_add_(0, seg, src)
def _pad(x, pw):
    flat = tuple(v for pair in reversed(pw) for v in pair)
    return torch.nn.functional.pad(x, flat)

kernel_name = 'gated_mix__mha_block_cached__dense_activation_squared_relu__decode'

def setup_inputs():

    _w = (lambda a: (lambda t: t.to(torch.bfloat16) if t.is_floating_point() else t)(torch.as_tensor(a)))
    def _A_setup():
        B, Sq, D = 2, 1, 448
        H, Sk, Dh = 16, 4096, 256
        return {
            "x": _w((lambda *s: torch.randn(*s, dtype=torch.bfloat16) * 0.05)(B, Sq, D)),
            "k_cache": _w((lambda *s: torch.randn(*s, dtype=torch.bfloat16) * 6.4)(B, H, Sk, Dh)),
            "v_cache": _w((lambda *s: torch.randn(*s, dtype=torch.bfloat16) * 3.2)(B, H, Sk, Dh)),
            "wq": _w((lambda *s: torch.randn(*s, dtype=torch.bfloat16) * 6.4)(D, H * Dh) * (D ** -0.5)),
            "wo": _w((lambda *s: torch.randn(*s, dtype=torch.bfloat16) * 3.2)(H * Dh, D) * ((H * Dh) ** -0.5)),
        }
    _A_in = _A_setup()

    def _B_setup():
        B, S, D, F = 2, 1, 448, 1792
        return {
            "x": _w((lambda *s: torch.randn(*s, dtype=torch.bfloat16) * 0.05)(B, S, D)),
            "w1": _w((lambda *s: torch.randn(*s, dtype=torch.bfloat16) * 0.2)(D, F)),
            "b1": _w((lambda *s: torch.randn(*s, dtype=torch.bfloat16) * 0.2)(F)),
            "w2": _w((lambda *s: torch.randn(*s, dtype=torch.bfloat16) * 0.2)(F, D)),
            "b2": _w((lambda *s: torch.randn(*s, dtype=torch.bfloat16) * 0.4)(D)),
        }
    _B_in = _B_setup()

    return {
        "A_x": _A_in["x"],
        "A_k_cache": _A_in["k_cache"],
        "A_v_cache": _A_in["v_cache"],
        "A_wq": _A_in["wq"],
        "A_wo": _A_in["wo"],
        "B_x": _B_in["x"],
        "B_w1": _B_in["w1"],
        "B_b1": _B_in["b1"],
        "B_w2": _B_in["w2"],
        "B_b2": _B_in["b2"],
        "gate_w": _w((lambda *s: torch.randn(*s, dtype=torch.bfloat16) * 0.1)(448) * 16.0),
    }

def reference(A_x, A_k_cache, A_v_cache, A_wq, A_wo, B_x, B_w1, B_b1, B_w2, B_b2, gate_w):
    def _A_ref(x, k_cache, v_cache, wq, wo):
        B, Sq, D = x.shape
        H, Dh = 16, 256
        q = (lambda x, *d: x.permute(*d))((x @ wq).reshape(B, Sq, H, Dh), 0, 2, 1, 3)
        scores = torch.einsum("bhqd,bhkd->bhqk", q, k_cache) * (Dh ** -0.5)
        p = _softmax(scores, axis=-1)
        o = torch.einsum("bhqk,bhkd->bhqd", p, v_cache)
        return (lambda x, *d: x.permute(*d))(o, 0, 2, 1, 3).reshape(B, Sq, H * Dh) @ wo

    def _B_ref(x, w1, b1, w2, b2):
        h = torch.einsum("bsd,df->bsf", x, w1) + b1
        zero = torch.zeros_like(h)
        r = torch.where(h > zero, h, zero)
        h = r * r
        out = torch.einsum("bsf,fd->bsd", h, w2) + b2
        return out

    x = A_x
    g = _sigmoid((x * gate_w).sum(-1, keepdims=True))
    a = _A_ref(x, A_k_cache, A_v_cache, A_wq, A_wo)
    b = _B_ref(x, B_w1, B_b1, B_w2, B_b2)
    return g * a + (1 - g) * b
```

```cpp
#include <hip/hip_runtime.h>
#include <stdint.h>
#include <utility>

#define GAS __attribute__((address_space(1)))
#define LAS __attribute__((address_space(3)))
typedef unsigned short bf16_t;
typedef unsigned u32x4 __attribute__((ext_vector_type(4)));
typedef unsigned u32x2 __attribute__((ext_vector_type(2)));
typedef float f32x4 __attribute__((ext_vector_type(4)));
typedef float f32x2 __attribute__((ext_vector_type(2)));
typedef __bf16 bf16x2 __attribute__((ext_vector_type(2)));
typedef __bf16 bf16x8 __attribute__((ext_vector_type(8)));
typedef short s16x4 __attribute__((ext_vector_type(4)));
typedef __amdgpu_buffer_rsrc_t rsrc_t;

constexpr int D = 448, SK = 4096, DH = 256, F = 1792;
constexpr int NTHR = 640;

#define TAU 452
#define ODELAY 132
#define YDELAY 220
constexpr int YOWN = 25;
constexpr int WFIRST = 50;
constexpr int INFL = 3;
constexpr int JREB = 9, REBX = 100;

constexpr int LAG = 8;
constexpr int JX = 2;
constexpr int JA = 9;
constexpr int JB = JA + 3;
constexpr int JG = 19;
constexpr int JQ = JG + 6;
constexpr int JK0 = JQ + 1;
constexpr int JH = 28;
constexpr int JS = 31 + LAG;
constexpr int JML = 50;
constexpr int kdone(int j) { return j < JK0 ? 0 : ((j - JK0 + 1) * 4 < j - LAG + 1 ? (j - JK0 + 1) * 4 : (j - LAG + 1 < 32 ? j - LAG + 1 : 32)); }
constexpr int vlag(int j) { return j < 60 ? LAG : (LAG - (j - 59) > 3 ? LAG - (j - 59) : 3); }
constexpr int vdone(int j) { return j <= JS ? 0 : (j >= 65 ? 32 : (j == 64 ? 31 : j - 31 - vlag(j))); }
constexpr int extras(int j) { return (j == -4) + (j == -3) + 4 * (j == JX) + (j == JREB) + (j == JA + 2) + (j == JB) + 2 * (j == JG) + (j == JH) + (j == JS + 2) + (j == JML); }
constexpr int inflight(int j) { int n = INFL; for (int k = j - INFL - 1; k < j; ++k) n += extras(k); return n; }
constexpr bool padat(int j) { return j == 12 || j == 14 || j == 18 || j == 25 || j == 27 || j == 31 || j == 36 || j == 39 || j == 41 || j == 44 || j == 50 || j == 54 || j == 58 || j == 60 || j == 65; }

constexpr int WS_EPOCH = 0;
constexpr int WS_G = 1024;
constexpr int WS_ML = 4096;
constexpr int WS_T0 = 8192;
constexpr int WS_QP = 65536;
constexpr int WS_HP = WS_QP + 32 * 8192;
constexpr int WS_O = WS_HP + 224 * 1024;
constexpr int WS_Y = WS_O + 256 * 1024;
constexpr int WS_END = WS_Y + 56 * 256 * 64;
constexpr unsigned TM = 0x3FFu;
constexpr int AUX_SC1 = 16, AUX_NT = 2;
constexpr int SPIN_CAP = 1 << 18;
constexpr int OOB = 0x7FFFFFF0;
constexpr int L_TP = 0;
constexpr int L_Q = 4096;
constexpr int L_T0 = L_Q + 128;
constexpr int L_ML = L_T0 + 4;
constexpr int L_F = L_ML + 16;
constexpr int OPS = 260;
constexpr int L_OP = L_F + 12;
constexpr int L_P = L_OP + 8 * OPS;
constexpr int CSL = 36;
constexpr int L_C = L_P + 512;
constexpr int L_W = L_C + 11 * CSL + 132;
constexpr int L_RED = L_W + 7 * 1056;
constexpr int LDS_BYTES = 84 * 1024;
static_assert(L_OP % 4 == 0 && L_P % 4 == 0 && L_C % 4 == 0 && L_W % 4 == 0 && (L_RED + 256) * 4 <= LDS_BYTES, "LDS layout");

__device__ __forceinline__ float asf(unsigned v) { return __builtin_bit_cast(float, v); }
__device__ __forceinline__ unsigned asu(float v) { return __builtin_bit_cast(unsigned, v); }
__device__ __forceinline__ float bflo(unsigned v) { return asf(v << 16); }
__device__ __forceinline__ float bfhi(unsigned v) { return asf(v & 0xffff0000u); }
__device__ __forceinline__ unsigned pk2(float lo, float hi) {
  f32x2 v = {lo, hi};
  return __builtin_bit_cast(unsigned, __builtin_convertvector(v, bf16x2));
}
__device__ __forceinline__ float dot2(unsigned a, unsigned b, float acc) {
  return __builtin_amdgcn_fdot2_f32_bf16(__builtin_bit_cast(bf16x2, a), __builtin_bit_cast(bf16x2, b), acc, false);
}
__device__ __forceinline__ unsigned perm_lo(unsigned a, unsigned b) { return __builtin_amdgcn_perm(b, a, 0x05040100u); }
__device__ __forceinline__ unsigned perm_hi(unsigned a, unsigned b) { return __builtin_amdgcn_perm(b, a, 0x07060302u); }
template <int CTRL> __device__ __forceinline__ float dpp(float x) {
  return __builtin_bit_cast(float, __builtin_amdgcn_mov_dpp(__builtin_bit_cast(int, x), CTRL, 0xf, 0xf, true));
}
constexpr int XOR1 = 0xB1, XOR2 = 0x4E, XOR7 = 0x141, XOR8 = 0x128, ROR4 = 0x124;
__device__ __forceinline__ float swap16_add(float a, float b) {
  auto s = __builtin_amdgcn_permlane16_swap(asu(a), asu(b), false, false);
  return asf(s[0]) + asf(s[1]);
}
__device__ __forceinline__ float swap32_add(float a, float b) {
  auto s = __builtin_amdgcn_permlane32_swap(asu(a), asu(b), false, false);
  return asf(s[0]) + asf(s[1]);
}
__device__ __forceinline__ float swap16_max(float a) {
  auto s = __builtin_amdgcn_permlane16_swap(asu(a), asu(a), false, false);
  return fmaxf(asf(s[0]), asf(s[1]));
}
__device__ __forceinline__ float swap32_max(float a) {
  auto s = __builtin_amdgcn_permlane32_swap(asu(a), asu(a), false, false);
  return fmaxf(asf(s[0]), asf(s[1]));
}
__device__ __forceinline__ float sum8(float v) { v += dpp<XOR1>(v); v += dpp<XOR2>(v); v += dpp<XOR7>(v); return v; }
__device__ __forceinline__ float max8(float v) { v = fmaxf(v, dpp<XOR1>(v)); v = fmaxf(v, dpp<XOR2>(v)); v = fmaxf(v, dpp<XOR7>(v)); return v; }
__device__ __forceinline__ float wave_sum(float v) { v = sum8(v); v += dpp<XOR8>(v); v = swap16_add(v, v); v = swap32_add(v, v); return v; }
__device__ __forceinline__ float wave_max(float v) { v = max8(v); v = fmaxf(v, dpp<XOR8>(v)); v = swap16_max(v); v = swap32_max(v); return v; }
#define DPP4(c) "v_add_f32_dpp %0, %0, %0 " c " row_mask:0xf bank_mask:0xf bound_ctrl:1\n\tv_add_f32_dpp %1, %1, %1 " c " row_mask:0xf bank_mask:0xf bound_ctrl:1\n\t" \
                "v_add_f32_dpp %2, %2, %2 " c " row_mask:0xf bank_mask:0xf bound_ctrl:1\n\tv_add_f32_dpp %3, %3, %3 " c " row_mask:0xf bank_mask:0xf bound_ctrl:1\n\t"
__device__ __forceinline__ f32x4 sum8x4(f32x4 t) {
  float a = t[0], b = t[1], c = t[2], d = t[3];
  asm volatile("s_nop 1\n\t" DPP4("quad_perm:[1,0,3,2]") DPP4("quad_perm:[2,3,0,1]") DPP4("row_half_mirror") : "+v"(a), "+v"(b), "+v"(c), "+v"(d));
  return f32x4{a, b, c, d};
}

__device__ __forceinline__ rsrc_t mk(const void* p, int bytes) { return __builtin_amdgcn_make_buffer_rsrc((void*)p, 0, bytes, 0x00020000); }
__device__ __forceinline__ u32x4 ldnt(rsrc_t r, int voff, int soff) { return __builtin_amdgcn_raw_buffer_load_b128(r, voff, soff, AUX_NT); }
__device__ __forceinline__ u32x4 ld_sc1(rsrc_t r, int voff) { return __builtin_amdgcn_raw_buffer_load_b128(r, voff, 0, AUX_SC1); }
__device__ __forceinline__ void st_sc1(u32x4 v, rsrc_t r, int voff) { __builtin_amdgcn_raw_buffer_store_b128(v, r, voff, 0, AUX_SC1); }
__device__ __forceinline__ u32x4 tagv(f32x4 t, u32x4 tg) {
  u32x4 u = __builtin_bit_cast(u32x4, t);
#pragma unroll
  for (int k = 0; k < 4; ++k) u[k] = (u[k] & ~TM) | tg[k];
  return u;
}
__device__ __forceinline__ bool clean(u32x4 e) { return ((e.x | e.y | e.z | e.w) & TM) == 0u; }

#define SB() __builtin_amdgcn_sched_barrier(0)
#define PIN(x) asm volatile("" : "+v"(x))
#define MEMB() asm volatile("" ::: "memory")
#define LDS_BAR() do { asm volatile("s_waitcnt lgkmcnt(0)" ::: "memory"); __builtin_amdgcn_s_barrier(); asm volatile("" ::: "memory"); } while (0)
#define RETRY(var, off, cond) do { if (__builtin_expect(!__all(cond), 0)) { _Pragma("nounroll") for (int sp_ = 0; sp_ < SPIN_CAP; ++sp_) { \
    __builtin_amdgcn_s_sleep(16); MEMB(); var = ld_sc1(rs, off); PIN(var); if (__all(cond)) break; } } } while (0)
#define PAD() asm volatile("9:\n\ts_cbranch_execz 9f")
#define SEG_ON(k) asm volatile("9:\n\ts_cbranch_execnz 8f\n\ts_branch " #k "f\n\t.p2align 12\n" #k ":\n\ts_branch 9f\n8:")
#define SEG_END(k) asm volatile("9:\n\ts_cbranch_execnz 8f\n\ts_endpgm\n\t.p2align 12\n" #k ":\n\ts_branch 9f\n8:")
#define PAD_END() asm volatile("9:\n\ts_cbranch_execnz 8f\n\ts_endpgm\n8:")

template <class Fn, int... I> __device__ __forceinline__ void static_for_impl(Fn&& f, std::integer_sequence<int, I...>) { (f(std::integral_constant<int, I>{}), ...); }
template <int N, class Fn> __device__ __forceinline__ void static_for(Fn&& f) { static_for_impl(f, std::make_integer_sequence<int, N>{}); }
__device__ __forceinline__ int pace(int due) {
  int late;
  asm volatile("1:\n\ts_memrealtime vcc\n\ts_waitcnt lgkmcnt(0)\n\ts_lshl_b32 vcc_lo, vcc_lo, 4\n\ts_sub_u32 %0, vcc_lo, %1\n\ts_cmp_lt_i32 %0, 0\n\ts_cbranch_scc1 1b"
               : "=&s"(late) : "s"(due) : "vcc", "scc");
  return late;
}

__global__ void __launch_bounds__(NTHR, 3)
fused(const bf16_t* __restrict__ x_, const bf16_t* __restrict__ K_, const bf16_t* __restrict__ V_, const bf16_t* __restrict__ wq_,
      const bf16_t* __restrict__ wo_, const bf16_t* __restrict__ w1_, const bf16_t* __restrict__ b1_, const bf16_t* __restrict__ w2_,
      const bf16_t* __restrict__ b2_, const bf16_t* __restrict__ gw_, bf16_t* __restrict__ out_, unsigned char* ws) {
  extern __shared__ __attribute__((aligned(16))) float smem[];
  LAS float* L = (LAS float*)smem;
  const int tid = threadIdx.x, lane = tid & 63, w = __builtin_amdgcn_readfirstlane(tid >> 6);
  asm volatile("s_cmp_lt_u32 %0, 8\n\ts_cbranch_scc1 7f\n\ts_mov_b64 exec, 0\n\ts_cmp_eq_u32 %0, 8\n\ts_cbranch_scc1 91f\n\ts_branch 93f\n7:" :: "s"(w) : "scc");
  const int half = lane >> 5, c = lane & 31, r8 = lane >> 3, ch = lane & 7, r4 = lane >> 4, c16 = lane & 15;
  const int bid = blockIdx.x, xcd = bid & 7, ii = bid >> 3;
  const int b = (ii >> 3) & 1, s = ii & 7, h = 2 * xcd + (ii >> 4), m16 = ii & 15;
  const int bh = b * 16 + h;
  const bool has_mlp = ii < 28;
  const int ug = xcd * 28 + (has_mlp ? ii : 0);
  const int cb1 = ug >> 4, rb1 = ug & 15;
  const int qcb = ii & 1, qrb = (ii >> 1) & 7;
  const bool gate_wg = ii == 28, b2_wg = bid == 29 * 8;
  const bool is_red = ii >= 25;
  const int red = is_red ? xcd * 7 + ii - 25 : 0;
  const int l16 = lane * 16;
  const bool w7 = w == 7;
  const int rot = (13 * (ii >> 3) + 12 * xcd) * 65536;
  int aoff = w7 ? (gate_wg && lane < 56 ? l16 : OOB) : (56 * qrb + 8 * w + 2 * r4) * 8192 + h * 512 + qcb * 256 + c16 * 16;
  PIN(aoff);
  SB();
  const rsrc_t ra = mk(w7 ? x_ : wq_, w7 ? 2 * D * 2 : D * 8192);
  const u32x4 ta0 = ldnt(ra, aoff, 0);
  SB();
  int t0s = (int)((unsigned)__builtin_amdgcn_s_memrealtime() << 4);
  asm volatile("" :: "s"(x_), "s"(K_), "s"(V_), "s"(wo_), "s"(w1_), "s"(b1_), "s"(w2_), "s"(b2_), "s"(gw_), "s"(out_), "s"(ws));
  const rsrc_t rs = mk(ws, WS_END);
  int due = t0s, step = TAU, dend;

  u32x4 kr[32], vr[32];
  f32x2 acc[4] = {{0.f, 0.f}, {0.f, 0.f}, {0.f, 0.f}, {0.f, 0.f}};
  float t0r[2], t1[2], t2[2], t3[2], t4[2];
  u32x4 ta1, tb, ep, tg, qv, ml, qg, hg, wr[3]; u32x2 tsm; f32x4 pp, hsq;
  float sv, mw, mown; unsigned xa0, xa1, xb0, xb1, tch;
  LAS f32x4 *lo_w, *lo_r; LAS float *lr_w, *lf_r; LAS f32x2* lr_r;
  int lof, lww, lwr, ogoff, ygoff, outoff, yoff, ooff, qgoff, hgoff, mloff;
  rsrc_t rk, rv;

  static_for<70>([&](auto jc) __attribute__((always_inline)) {
    constexpr int j = decltype(jc)::value - 4;
    if constexpr (j == 11) SEG_ON(91); else if constexpr (j == 28) SEG_ON(92); else if constexpr (j == 46) SEG_END(93); else if constexpr (j == 63) SEG_ON(94);
    else if constexpr (padat(j)) PAD();
    if constexpr (j >= INFL - 3) asm volatile("s_waitcnt vmcnt(%0)" :: "i"(inflight(j)));
    if constexpr (j == 65) PIN(vr[31]);
    else if constexpr (j > -4) {
      const int late = pace(due);
      if constexpr (j >= INFL - 3 && j < JREB) { if (late > REBX) { due += late - REBX; t0s += late - REBX; } }
    }
    if constexpr (j == 63) dend = due;
    due += j < -2 ? TAU * 56 / 64 : j < 0 ? TAU * 50 / 64 : j < 63 ? step : WFIRST * 16;
    SB();
    if constexpr (j == -4) {}
    else if constexpr (j == -3) ta1 = ldnt(ra, aoff, w7 ? D * 2 : 8192);
    else if constexpr (j == -2) tb = ldnt(w7 ? (gate_wg ? mk(gw_, 896) : mk(b1_ + cb1 * 128, has_mlp && rb1 == 0 ? 256 : 0))
                                             : mk(w1_ + (size_t)(28 * rb1 + 4 * w) * F + cb1 * 128, has_mlp ? 4 * F * 2 : 0), w7 ? l16 : r4 * (F * 2) + c16 * 16, 0);
    else if constexpr (j == -1) wr[2] = ldnt(mk((b2_wg ? b2_ : w2_ + (size_t)(ug * 8) * D) + w * 64, w7 ? 0 : (has_mlp ? 8 * 896 - w * 128 : (b2_wg ? 128 : 0))), r8 * 896 + ch * 16, 0);
    else if constexpr (j < 32) kr[j] = ldnt(rk, l16, (j * 65536 + rot) & 0x1F0000);
    else if constexpr (j < 64) vr[j - 32] = ldnt(rv, l16, (j * 65536 + rot) & 0x1F0000);
    else wr[j - 64] = ldnt(mk(wo_ + (size_t)(h * 256 + 16 * m16) * D + w * 64, w7 ? 0 : 16 * 896 - w * 128), r8 * 896 + ch * 16, (j - 64) * 8 * 896);
    SB();
    if constexpr (j == -4) {
      rk = mk(K_ + ((size_t)bh * SK + (w * 8 + s) * 2) * DH, 31 * 65536 + 1024);
      rv = mk(V_ + ((size_t)bh * SK + (w * 8 + s) * 2) * DH, 31 * 65536 + 1024);
      tch = 0;
      if (w7) tch = __builtin_nontemporal_load((const GAS unsigned*)(lane == 5 && is_red ? (const bf16_t*)out_ : x_));
    }
    if constexpr (j == -3) ep = ld_sc1(rs, WS_EPOCH);
    if constexpr (j == JX) {
      const rsrc_t rx = mk(x_, w7 ? 0 : 2 * D * 2);
      xa0 = __builtin_amdgcn_raw_buffer_load_b32(rx, r4 * 4, (28 * qrb + 4 * w) * 4, 0);
      xa1 = __builtin_amdgcn_raw_buffer_load_b32(rx, r4 * 4, (28 * qrb + 4 * w) * 4 + D * 2, 0);
      xb0 = __builtin_amdgcn_raw_buffer_load_b16(rx, r4 * 2, (28 * rb1 + 4 * w) * 2, 0);
      xb1 = __builtin_amdgcn_raw_buffer_load_b16(rx, r4 * 2, (28 * rb1 + 4 * w) * 2 + D * 2, 0);
    }
    if constexpr (j == JA) PIN(tch);
    if constexpr (j == JREB) __builtin_amdgcn_raw_buffer_store_b64(u32x2{(unsigned)t0s, ep.x + 1u}, rs, (tid == 192 && ii % 3 == 1 && ii < 24) ? WS_T0 + (xcd * 8 + ii / 3) * 8 : OOB, 0, AUX_SC1);
    if constexpr (j == JG) { qg = ld_sc1(rs, qgoff); tsm = __builtin_amdgcn_raw_buffer_load_b64(rs, w7 ? WS_T0 + lane * 8 : OOB, 0, AUX_SC1); }
    if constexpr (j == JH) hg = ld_sc1(rs, w7 ? hgoff : OOB);
    if constexpr (j == JML) ml = ld_sc1(rs, w7 ? mloff : OOB);
    SB();
    if constexpr (j == JA) {
      float a[8];
#pragma unroll
      for (int k = 0; k < 4; ++k) {
        const unsigned lo = perm_lo(ta0[k], ta1[k]), hi = perm_hi(ta0[k], ta1[k]);
        a[2 * k] = swap32_add(dot2(lo, xa0, 0.f), dot2(lo, xa1, 0.f));
        a[2 * k + 1] = swap32_add(dot2(hi, xa0, 0.f), dot2(hi, xa1, 0.f));
      }
      f32x4 t;
#pragma unroll
      for (int k = 0; k < 4; ++k) t[k] = swap16_add(a[k], a[k + 4]);
      *(LAS f32x4*)(L + L_TP + w * 256 + half * 128 + c16 * 8 + (r4 & 1) * 4) = t;
    }
    if constexpr (j == JA + 1) {
      float e[8];
      const float f0 = w7 ? 1.f : bflo(xb0), f1 = w7 ? 1.f : bflo(xb1);
#pragma unroll
      for (int k = 0; k < 4; ++k) {
        e[2 * k] = swap32_add(bflo(tb[k]) * f0, bflo(tb[k]) * f1);
        e[2 * k + 1] = swap32_add(bfhi(tb[k]) * f0, bfhi(tb[k]) * f1);
      }
      f32x4 t;
#pragma unroll
      for (int k = 0; k < 4; ++k) t[k] = swap16_add(e[k], e[k + 4]);
      *(LAS f32x4*)(L + L_TP + 2048 + w * 256 + half * 128 + c16 * 8 + (r4 & 1) * 4) = t;
    }
    if constexpr (j == JA + 2) {
      if (tid < 11 * CSL) L[L_C + tid] = 0.f;
      ep += 1u; tg = ep & TM;
      PIN(ep); PIN(tg);
      if (w7) {
        float ga = 0.f, gb = 0.f;
#pragma unroll
        for (int k = 0; k < 4; ++k) { ga = dot2(ta0[k], tb[k], ga); gb = dot2(ta1[k], tb[k], gb); }
        ga = 1.f / (1.f + __expf(-wave_sum(ga))); gb = 1.f / (1.f + __expf(-wave_sum(gb)));
        st_sc1(u32x4{asu(ga), ep.x, asu(gb), ep.y}, rs, gate_wg && lane == 0 ? WS_G + xcd * 128 : OOB);
      }
    }
    if constexpr (j == JB) {
      LDS_BAR();
      if (w < 2) {
        f32x4 t = *(LAS f32x4*)(L + L_TP + w * 2048 + 4 * lane);
#pragma unroll
        for (int k = 1; k < 7; ++k) t += *(LAS f32x4*)(L + L_TP + w * 2048 + k * 256 + 4 * lane);
        if (w == 1) t += *(LAS f32x4*)(L + L_TP + 2048 + 7 * 256 + 4 * lane);
        st_sc1(tagv(t, tg), rs, w == 0 ? WS_QP + (h * 2 + half) * 8192 + qrb * 1024 + qcb * 512 + c * 16 : (has_mlp ? WS_HP + ug * 1024 + l16 : OOB));
      }
    }
    if constexpr (j == 5) {
      qgoff = WS_QP + (h * 2 + b) * 8192 + ch * 1024 + w * 128 + r8 * 16;
      hgoff = WS_HP + (cb1 * 16 + c16) * 1024 + (r4 & 1) * 512 + rb1 * 32 + half * 16;
      mloff = (lane & 8) ? WS_G + xcd * 128 : WS_ML + bh * 128 + ch * 16;
      PIN(qgoff); PIN(hgoff); PIN(mloff);
    }
    if constexpr (j == JB + 1) {
      lo_w = (LAS f32x4*)(L + L_OP + w * OPS + 8 * c + 4 * half);
      lo_r = (LAS f32x4*)(L + L_OP + ch * OPS + 32 * w + 4 * r8);
      lf_r = L + L_F + ch;
      lr_w = L + L_RED + ((lane & 12) == 0 ? w * 18 + (lane & 3) * 4 + half * 2 + (r4 & 1) : 160 + lane);
      lr_r = (LAS f32x2*)(L + L_RED + ch * 18 + 2 * r8);
      PIN(lo_w); PIN(lo_r); PIN(lf_r); PIN(lr_w); PIN(lr_r);
    }
    if constexpr (j == JB + 2) {
      lof = (int)(size_t)(L + L_C + (w7 ? (ch == 0 ? 7 * CSL + (r8 & 1) * 16 + r4 * 2 : 11 * CSL + lane * 2) : (7 - (c16 >> 1)) * CSL + (lane & 1) * 16 + r4 * 4));
      lww = (int)(size_t)(L + L_W + w * 1056) + r8 * 160 + ch * 16;
      lwr = (int)(size_t)(L + L_W + w * 1056) + (r4 < 2 ? r4 : 2) * 1408 + (c16 >> 2) * 160 + (lane & 3) * 8;
      PIN(lof); PIN(lww); PIN(lwr);
      if (!w7) *(LAS u32x4*)(size_t)(lww + 2 * 1408) = wr[2];
    }
    if constexpr (j == JB + 3) {
      ogoff = WS_O + (h * 16 + m16) * 1024 + c16 * 64 + r4 * 16;
      ygoff = WS_Y + red * 16384 + w * 2048 + l16;
      outoff = ch == 0 ? (red * 8 + r8) * 4 : OOB;
      PIN(ogoff); PIN(ygoff); PIN(outoff);
    }
    if constexpr (j == JB + 4) {
      yoff = (lane & 8) == 0 ? WS_Y + (((lane & 1) * 28 + 4 * w + (ch >> 1)) * 256 + xcd * 32 + ii) * 64 + r4 * 16 : OOB;
      ooff = ch == 0 ? WS_O + (h * 16 + 2 * w + (r8 >> 2)) * 1024 + m16 * 64 + (r8 & 3) * 16 : OOB;
      PIN(yoff); PIN(ooff);
    }
    if constexpr (j == JQ - 1) {
      PIN(tsm);
      if (w7) {
        const float d = wave_sum(tsm.y == ep.x ? (float)(int)(tsm.x - (unsigned)t0s) : 0.f) * (1.f / 64.f);
        if (lane == 0) ((LAS unsigned*)L)[L_T0] = (unsigned)t0s + (unsigned)(int)d;
      }
    }
    if constexpr (j == JQ) {
      PIN(qg);
      RETRY(qg, qgoff, clean(qg ^ tg));
      qg ^= tg;
      const u32x2 qq = {pk2(sum8(asf(qg.x)), sum8(asf(qg.y))), pk2(sum8(asf(qg.z)), sum8(asf(qg.w)))};
      if (ch == 0) *(LAS u32x2*)(L + L_Q + w * 16 + r8 * 2) = qq;
    }
    if constexpr (j == JK0) {
      LDS_BAR();
      qv = *(LAS u32x4*)(L + L_Q + 4 * c);
      const float st = (float)(int)(((LAS unsigned*)L)[L_T0] - (unsigned)t0s) * (1.f / (62 - JK0));
      int sti = (int)__builtin_rintf(fminf(fmaxf(st, -100.f), 40.f)); PIN(sti);
      step = TAU + __builtin_amdgcn_readfirstlane(sti);
    }
    if constexpr (j == JH + LAG) {
      PIN(hg);
      if (w7) RETRY(hg, hgoff, clean(hg ^ tg));
      hg ^= tg;
#pragma unroll
      for (int k = 0; k < 4; ++k) {
        float r = sum8(asf(hg[k])); r += dpp<XOR8>(r);
        r = fmaxf(r, 0.f);
        hsq[k] = b2_wg ? 1.f : r * r;
      }
      PIN(hsq);
    }
#pragma unroll
    for (int i = kdone(j - 1); i < kdone(j); ++i) {
      const u32x4 kv = kr[i];
      float d = dot2(kv.x, qv.x, 0.f); d = dot2(kv.y, qv.y, d); d = dot2(kv.z, qv.z, d); d = dot2(kv.w, qv.w, d);
      t0r[i & 1] = d;
      if ((i & 1) == 0) PIN(t0r[0]);
      if ((i & 1) == 1) { t1[(i >> 1) & 1] = swap16_add(t0r[0], t0r[1]); PIN(t1[(i >> 1) & 1]); }
      if ((i & 3) == 3) { const float a = t1[0] + dpp<XOR8>(t1[0]), bb = t1[1] + dpp<XOR8>(t1[1]); t2[(i >> 2) & 1] = (lane & 8) ? bb : a; PIN(t2[(i >> 2) & 1]); }
      if ((i & 7) == 7) { const float a = t2[0] + dpp<XOR7>(t2[0]), bb = t2[1] + dpp<XOR7>(t2[1]); t3[(i >> 3) & 1] = (lane & 4) ? bb : a; PIN(t3[(i >> 3) & 1]); }
      if ((i & 15) == 15) { const float a = t3[0] + dpp<XOR2>(t3[0]), bb = t3[1] + dpp<XOR2>(t3[1]); t4[(i >> 4) & 1] = (lane & 2) ? bb : a; PIN(t4[(i >> 4) & 1]); }
    }
    if constexpr (j == JS) {
      const float a = t4[0] + dpp<XOR1>(t4[0]), bb = t4[1] + dpp<XOR1>(t4[1]);
      sv = (lane & 1) ? bb : a;
      sv = bflo(pk2(sv, sv)) * (0.0625f * 1.4426950408889634f);
      mw = wave_max(sv);
      sv = __builtin_amdgcn_exp2f(sv - mw);
      L[L_P + w * 64 + half * 32 + (int)(__builtin_bitreverse32((unsigned)c) >> 27)] = sv;
      PIN(sv); PIN(mw);
    }
    if constexpr (j == JS + 1) {
      const float lw = wave_sum(sv);
      if (lane == 0) *(LAS f32x2*)(L + L_ML + 2 * w) = f32x2{mw, lw};
    }
    if constexpr (j == JS + 2) {
      LDS_BAR();
      const f32x2 t = *(LAS f32x2*)(L + L_ML + 2 * ch);
      mown = t[0];
      const float M = max8(mown);
      const float Lt = sum8(t[1] * __builtin_amdgcn_exp2f(mown - M));
      st_sc1(u32x4{asu(M), ep.x, asu(Lt), ep.y}, rs, tid == 448 ? WS_ML + bh * 128 + s * 16 : OOB);
      PIN(mown);
    }
    if constexpr (j == JML + LAG) {
      PIN(ml);
      if (w7) {
        RETRY(ml, mloff, ml.y == ep.x && ml.w == ep.y);
        const float g0 = dpp<XOR8>(asf(ml.x)), g1 = dpp<XOR8>(asf(ml.z));
        const f32x4 t = hsq * (1.f - ((lane & 16) ? g1 : g0));
        if (c16 == 0) *(LAS u32x2*)(L + L_C + 7 * CSL + (r4 & 1) * 16 + 8 + half * 2) = u32x2{pk2(t[0], t[1]), pk2(t[2], t[3])};
        const float mi = asf(ml.x), li = asf(ml.z);
        const float M = max8(mi);
        const float Lt = sum8(li * __builtin_amdgcn_exp2f(mi - M));
        const float f = (b ? g1 : g0) * __builtin_amdgcn_exp2f(mown - M) / Lt;
        if (lane < 8) *lf_r = f;
      }
    }
#pragma unroll
    for (int i = vdone(j - 1); i < vdone(j); ++i) {
      if ((i & 3) == 0) { pp = *(LAS f32x4*)(L + L_P + w * 64 + half * 32 + i); PIN(pp); }
      const u32x4 vv = vr[i];
      const f32x2 p2 = {pp[i & 3], pp[i & 3]};
#pragma unroll
      for (int k = 0; k < 4; ++k) { const f32x2 v2 = {bflo(vv[k]), bfhi(vv[k])}; acc[k] = __builtin_elementwise_fma(p2, v2, acc[k]); PIN(acc[k]); }
    }
    SB();
  });

  *lo_w = f32x4{swap32_add(acc[0][0], acc[2][0]), swap32_add(acc[0][1], acc[2][1]), swap32_add(acc[1][0], acc[3][0]), swap32_add(acc[1][1], acc[3][1])};
  LDS_BAR();
  st_sc1(tagv(sum8x4(*lo_r * *lf_r), tg), rs, ooff);
  SB();
  if (w7) {
    PAD();
    pace(dend + ODELAY * 16);
    u32x4 og;
#pragma nounroll
    for (int sp = 0; sp < SPIN_CAP; ++sp) {
      MEMB();
      og = ld_sc1(rs, ogoff) ^ tg;
      if (__all(clean(og))) break;
    }
    const f32x4 t = sum8x4(__builtin_bit_cast(f32x4, og));
    *(LAS u32x2*)(size_t)lof = u32x2{pk2(t[0], t[1]), pk2(t[2], t[3])};
    LDS_BAR();
  } else {
    PAD();
    *(LAS u32x4*)(size_t)lww = wr[0];
    *(LAS u32x4*)(size_t)(lww + 1408) = wr[1];
    MEMB();
    u32x4 am[4], bm[4];
#pragma unroll
    for (int m = 0; m < 4; ++m) {
      const u32x2 lo = __builtin_bit_cast(u32x2, __builtin_amdgcn_ds_read_tr16_b64_v4i16((LAS s16x4*)(size_t)(lwr + m * 32)));
      const u32x2 hi = __builtin_bit_cast(u32x2, __builtin_amdgcn_ds_read_tr16_b64_v4i16((LAS s16x4*)(size_t)(lwr + m * 32 + 640)));
      am[m] = u32x4{lo.x, lo.y, hi.x, hi.y};
    }
    SB();
    LDS_BAR();
#pragma unroll
    for (int m = 0; m < 4; ++m) bm[m] = *(LAS u32x4*)(size_t)(lof + m * CSL * 4);
    SB();
    f32x4 t = {0.f, 0.f, 0.f, 0.f};
#pragma unroll
    for (int m = 0; m < 4; ++m) t = __builtin_amdgcn_mfma_f32_16x16x32_bf16(__builtin_bit_cast(bf16x8, am[m]), __builtin_bit_cast(bf16x8, bm[m]), t, 0, 0, 0);
    st_sc1(tagv(t, tg), rs, yoff);
  }
  if (is_red) {
    PAD();
    const int own = (int)((unsigned)__builtin_amdgcn_s_memrealtime() << 4) + (w7 ? YOWN + 10 : YOWN) * 16, plan = dend + YDELAY * 16;
    pace((int)((unsigned)own - (unsigned)plan) > 0 ? own : plan);
    u32x4 ya, yb;
#pragma nounroll
    for (int sp = 0; sp < SPIN_CAP; ++sp) {
      MEMB();
      ya = ld_sc1(rs, ygoff) ^ tg;
      yb = ld_sc1(rs, ygoff + 1024) ^ tg;
      if (__all(clean(ya | yb))) break;
    }
    const f32x4 t = __builtin_bit_cast(f32x4, ya) + __builtin_bit_cast(f32x4, yb);
    float r = swap16_add(swap32_add(t[0], t[2]), swap32_add(t[1], t[3]));
    r += dpp<XOR8>(r); r += dpp<ROR4>(r);
    *lr_w = r;
    LDS_BAR();
    if (w == 0) {
      const f32x2 u = *lr_r;
      __builtin_amdgcn_raw_buffer_store_b32(pk2(sum8(u[0]), sum8(u[1])), mk(out_, 2 * D * 2), outoff, 0, 0);
    }
    if (red == 0 && w == 1) st_sc1(ep, rs, lane == 0 ? WS_EPOCH : OOB);
  }
  PAD_END();
}

extern "C" void kernel_launch(void* const* d_in, const int* in_sizes, int n_in, void* d_out, int out_size, void* d_ws, size_t ws_size, hipStream_t stream) {
  (void)hipFuncSetAttribute((const void*)fused, hipFuncAttributeMaxDynamicSharedMemorySize, LDS_BYTES);
  fused<<<256, NTHR, LDS_BYTES, stream>>>((const bf16_t*)d_in[0], (const bf16_t*)d_in[1], (const bf16_t*)d_in[2], (const bf16_t*)d_in[3], (const bf16_t*)d_in[4],
                                          (const bf16_t*)d_in[6], (const bf16_t*)d_in[7], (const bf16_t*)d_in[8], (const bf16_t*)d_in[9], (const bf16_t*)d_in[10],
                                          (bf16_t*)d_out, (unsigned char*)d_ws);
}
```

```cpp
#include <hip/hip_runtime.h>
#include <stdint.h>
#include <utility>

#define GAS __attribute__((address_space(1)))
#define LAS __attribute__((address_space(3)))
typedef unsigned short bf16_t;
typedef unsigned u32x4 __attribute__((ext_vector_type(4)));
typedef unsigned u32x2 __attribute__((ext_vector_type(2)));
typedef float f32x4 __attribute__((ext_vector_type(4)));
typedef float f32x2 __attribute__((ext_vector_type(2)));
typedef __bf16 bf16x2 __attribute__((ext_vector_type(2)));
typedef __bf16 bf16x8 __attribute__((ext_vector_type(8)));
typedef short s16x4 __attribute__((ext_vector_type(4)));
typedef unsigned long long u64x8 __attribute__((ext_vector_type(8)));
typedef unsigned long long u64x4 __attribute__((ext_vector_type(4)));
typedef __amdgpu_buffer_rsrc_t rsrc_t;

constexpr int D = 448, SK = 4096, DH = 256, F = 1792;
constexpr int NTHR = 640;

#define TAU 445
#define ODELAY 130
#define YDELAY 216
constexpr int YOWN = 25;
constexpr int WFIRST = 50;
constexpr int INFL = 3;
constexpr int JREB = 9, REBX = 100;

constexpr int LAG = 8;
constexpr int JX = 2;
constexpr int JA = 9;
constexpr int JB = JA + 3;
constexpr int JG = 19;
constexpr int JQ = JG + 6;
constexpr int JK0 = JQ + 1;
constexpr int JH = 28;
constexpr int JS = 31 + LAG;
constexpr int JML = 50;
constexpr int kdone(int j) { return j < JK0 ? 0 : ((j - JK0 + 1) * 4 < j - LAG + 1 ? (j - JK0 + 1) * 4 : (j - LAG + 1 < 32 ? j - LAG + 1 : 32)); }
constexpr int vlag(int j) { return j < 60 ? LAG : (LAG - (j - 59) > 3 ? LAG - (j - 59) : 3); }
constexpr int vdone(int j) { return j <= JS ? 0 : (j >= 65 ? 32 : (j == 64 ? 31 : j - 31 - vlag(j))); }
constexpr int extras(int j) { return (j == -5) + (j == -4) + 4 * (j == JX) + (j == JREB) + (j == JA + 2) + (j == JB) + 2 * (j == JG) + (j == JH) + (j == JS + 2) + (j == JML); }
constexpr int inflight(int j) {
  const int f = j < 3 ? INFL + 2 : j < 5 ? INFL + 1 : INFL; int n = f;
  for (int k = j - f - 1; k < j; ++k) n += extras(k);
  return n;
}
constexpr bool padat(int j) { return j == 12 || j == 14 || j == 18 || j == 25 || j == 27 || j == 31 || j == 36 || j == 39 || j == 41 || j == 44 || j == 50 || j == 54 || j == 58 || j == 60 || j == 65; }

constexpr int WS_EPOCH = 0;
constexpr int WS_G = 1024;
constexpr int WS_ML = 4096;
constexpr int WS_T0 = 8192;
constexpr int WS_QP = 65536;
constexpr int WS_HP = WS_QP + 32 * 4096;
constexpr int WS_O = WS_HP + 56 * 1024;
constexpr int WS_Y = WS_O + 256 * 1024;
constexpr int WS_END = WS_Y + 56 * 256 * 64;
constexpr unsigned TM = 0x3FFu;
constexpr unsigned TMQ = 0xFu;
constexpr int AUX_SC1 = 16, AUX_NT = 2;
constexpr int SPIN_CAP = 1 << 18;
constexpr int OOB = 0x7FFFFFF0;
constexpr int L_TP = 0;
constexpr int L_Q = 2048;
constexpr int L_T0 = L_Q + 128;
constexpr int L_ML = L_T0 + 4;
constexpr int L_F = L_ML + 16;
constexpr int OPS = 260;
constexpr int L_OP = L_F + 12;
constexpr int L_P = L_OP + 8 * OPS;
constexpr int CSL = 36;
constexpr int L_C = L_P + 512;
constexpr int L_W = L_C + 11 * CSL + 132;
constexpr int L_RED = L_W + 7 * 1056;
constexpr int LDS_BYTES = 84 * 1024;
static_assert(L_OP % 4 == 0 && L_P % 4 == 0 && L_C % 4 == 0 && L_W % 4 == 0 && (L_RED + 256) * 4 <= LDS_BYTES, "LDS layout");

__device__ __forceinline__ float asf(unsigned v) { return __builtin_bit_cast(float, v); }
__device__ __forceinline__ unsigned asu(float v) { return __builtin_bit_cast(unsigned, v); }
__device__ __forceinline__ float bflo(unsigned v) { return asf(v << 16); }
__device__ __forceinline__ float bfhi(unsigned v) { return asf(v & 0xffff0000u); }
__device__ __forceinline__ unsigned pk2(float lo, float hi) {
  f32x2 v = {lo, hi};
  return __builtin_bit_cast(unsigned, __builtin_convertvector(v, bf16x2));
}
__device__ __forceinline__ float dot2(unsigned a, unsigned b, float acc) {
  return __builtin_amdgcn_fdot2_f32_bf16(__builtin_bit_cast(bf16x2, a), __builtin_bit_cast(bf16x2, b), acc, false);
}
__device__ __forceinline__ unsigned perm_lo(unsigned a, unsigned b) { return __builtin_amdgcn_perm(b, a, 0x05040100u); }
__device__ __forceinline__ unsigned perm_hi(unsigned a, unsigned b) { return __builtin_amdgcn_perm(b, a, 0x07060302u); }
template <int CTRL> __device__ __forceinline__ float dpp(float x) {
  return __builtin_bit_cast(float, __builtin_amdgcn_mov_dpp(__builtin_bit_cast(int, x), CTRL, 0xf, 0xf, true));
}
constexpr int XOR1 = 0xB1, XOR2 = 0x4E, XOR7 = 0x141, XOR8 = 0x128, ROR4 = 0x124;
__device__ __forceinline__ float swap16_add(float a, float b) {
  auto s = __builtin_amdgcn_permlane16_swap(asu(a), asu(b), false, false);
  return asf(s[0]) + asf(s[1]);
}
__device__ __forceinline__ float swap32_add(float a, float b) {
  auto s = __builtin_amdgcn_permlane32_swap(asu(a), asu(b), false, false);
  return asf(s[0]) + asf(s[1]);
}
__device__ __forceinline__ float swap16_max(float a) {
  auto s = __builtin_amdgcn_permlane16_swap(asu(a), asu(a), false, false);
  return fmaxf(asf(s[0]), asf(s[1]));
}
__device__ __forceinline__ float swap32_max(float a) {
  auto s = __builtin_amdgcn_permlane32_swap(asu(a), asu(a), false, false);
  return fmaxf(asf(s[0]), asf(s[1]));
}
__device__ __forceinline__ float sum4(float v) { v += dpp<XOR1>(v); v += dpp<XOR2>(v); return v; }
__device__ __forceinline__ float sum8(float v) { v += dpp<XOR1>(v); v += dpp<XOR2>(v); v += dpp<XOR7>(v); return v; }
__device__ __forceinline__ float max8(float v) { v = fmaxf(v, dpp<XOR1>(v)); v = fmaxf(v, dpp<XOR2>(v)); v = fmaxf(v, dpp<XOR7>(v)); return v; }
__device__ __forceinline__ float wave_sum(float v) { v = sum8(v); v += dpp<XOR8>(v); v = swap16_add(v, v); v = swap32_add(v, v); return v; }
__device__ __forceinline__ float wave_max(float v) { v = max8(v); v = fmaxf(v, dpp<XOR8>(v)); v = swap16_max(v); v = swap32_max(v); return v; }
#define DPP4(c) "v_add_f32_dpp %0, %0, %0 " c " row_mask:0xf bank_mask:0xf bound_ctrl:1\n\tv_add_f32_dpp %1, %1, %1 " c " row_mask:0xf bank_mask:0xf bound_ctrl:1\n\t" \
                "v_add_f32_dpp %2, %2, %2 " c " row_mask:0xf bank_mask:0xf bound_ctrl:1\n\tv_add_f32_dpp %3, %3, %3 " c " row_mask:0xf bank_mask:0xf bound_ctrl:1\n\t"
__device__ __forceinline__ f32x4 sum8x4(f32x4 t) {
  float a = t[0], b = t[1], c = t[2], d = t[3];
  asm volatile("s_nop 1\n\t" DPP4("quad_perm:[1,0,3,2]") DPP4("quad_perm:[2,3,0,1]") DPP4("row_half_mirror") : "+v"(a), "+v"(b), "+v"(c), "+v"(d));
  return f32x4{a, b, c, d};
}

__device__ __forceinline__ rsrc_t mk(const void* p, int bytes) { return __builtin_amdgcn_make_buffer_rsrc((void*)p, 0, bytes, 0x00020000); }
__device__ __forceinline__ u32x4 ldnt(rsrc_t r, int voff, int soff) { return __builtin_amdgcn_raw_buffer_load_b128(r, voff, soff, AUX_NT); }
__device__ __forceinline__ u32x4 ld_sc1(rsrc_t r, int voff) { return __builtin_amdgcn_raw_buffer_load_b128(r, voff, 0, AUX_SC1); }
__device__ __forceinline__ void st_sc1(u32x4 v, rsrc_t r, int voff) { __builtin_amdgcn_raw_buffer_store_b128(v, r, voff, 0, AUX_SC1); }
__device__ __forceinline__ u32x4 tagv(f32x4 t, u32x4 tg) {
  u32x4 u = __builtin_bit_cast(u32x4, t);
#pragma unroll
  for (int k = 0; k < 4; ++k) u[k] = (u[k] & ~TM) | tg[k];
  return u;
}
__device__ __forceinline__ u32x4 tagq(f32x4 t, u32x4 tg) {
  u32x4 u = __builtin_bit_cast(u32x4, t);
#pragma unroll
  for (int k = 0; k < 4; ++k) u[k] = ((u[k] + (TMQ + 1) / 2) & ~TMQ) | (tg[k] & TMQ);
  return u;
}
__device__ __forceinline__ bool clean(u32x4 e, unsigned m) { return ((e.x | e.y | e.z | e.w) & m) == 0u; }

#define SB() __builtin_amdgcn_sched_barrier(0)
#define PIN(x) asm volatile("" : "+v"(x))
#define MEMB() asm volatile("" ::: "memory")
#define LDS_BAR() do { asm volatile("s_waitcnt lgkmcnt(0)" ::: "memory"); __builtin_amdgcn_s_barrier(); asm volatile("" ::: "memory"); } while (0)
#define RETRY(var, off, cond) do { if (__builtin_expect(!__all(cond), 0)) { _Pragma("nounroll") for (int sp_ = 0; sp_ < SPIN_CAP; ++sp_) { \
    __builtin_amdgcn_s_sleep(16); MEMB(); var = ld_sc1(rs, off); PIN(var); if (__all(cond)) break; } } } while (0)
#define PAD() asm volatile("9:\n\ts_cbranch_execz 9f")
#define SEG_ON(k) asm volatile("9:\n\ts_cbranch_execnz 8f\n\ts_branch " #k "f\n\t.p2align 12\n" #k ":\n\ts_branch 9f\n8:")
#define SEG_END(k) asm volatile("9:\n\ts_cbranch_execnz 8f\n\ts_endpgm\n\t.p2align 12\n" #k ":\n\ts_branch 9f\n8:")
#define PAD_END() asm volatile("9:\n\ts_cbranch_execnz 8f\n\ts_endpgm\n8:")

template <class Fn, int... I> __device__ __forceinline__ void static_for_impl(Fn&& f, std::integer_sequence<int, I...>) { (f(std::integral_constant<int, I>{}), ...); }
template <int N, class Fn> __device__ __forceinline__ void static_for(Fn&& f) { static_for_impl(f, std::make_integer_sequence<int, N>{}); }
__device__ __forceinline__ int pace(int due) {
  int late;
  asm volatile("1:\n\ts_memrealtime vcc\n\ts_waitcnt lgkmcnt(0)\n\ts_lshl_b32 vcc_lo, vcc_lo, 4\n\ts_sub_u32 %0, vcc_lo, %1\n\ts_cmp_lt_i32 %0, 0\n\ts_cbranch_scc1 1b"
               : "=&s"(late) : "s"(due) : "vcc", "scc");
  return late;
}

__global__ void __launch_bounds__(NTHR, 3)
fused(const bf16_t* x, const bf16_t* k_cache, const bf16_t* v_cache, const bf16_t* wq, const bf16_t* wo, const bf16_t* w1, const bf16_t* b1, const bf16_t* w2,
      const bf16_t* b2, const bf16_t* gate_w, bf16_t* out, unsigned char* workspace) {
  u64x8 ka; u64x4 kb;
  asm volatile("s_load_dwordx16 %0, %2, 0x0\n\ts_load_dwordx8 %1, %2, 0x40" : "=&s"(ka), "=&s"(kb) : "s"(__builtin_amdgcn_kernarg_segment_ptr()));
  SB();
  extern __shared__ __attribute__((aligned(16))) float smem[];
  LAS float* L = (LAS float*)smem;
  const int tid = threadIdx.x, lane = tid & 63, w = __builtin_amdgcn_readfirstlane(tid >> 6);
  asm volatile("s_cmp_lt_u32 %0, 8\n\ts_cbranch_scc1 7f\n\ts_mov_b64 exec, 0\n\ts_cmp_eq_u32 %0, 8\n\ts_cbranch_scc1 91f\n\ts_branch 93f\n7:" :: "s"(w) : "scc");
  const int half = lane >> 5, c = lane & 31, r8 = lane >> 3, ch = lane & 7, r4 = lane >> 4, c16 = lane & 15;
  const int bid = blockIdx.x, xcd = bid & 7, ii = bid >> 3;
  const int b = (ii >> 3) & 1, s = ii & 7, h = 2 * xcd + (ii >> 4), m16 = ii & 15;
  const int bh = b * 16 + h;
  const int rot = (13 * (ii >> 3) + 12 * xcd) * 65536;
  u32x4 kr[32], vr[32];
  int kvoff = (bh * 2048 + w * 8 + s) * 1024 + lane * 16, rot0 = rot & 0x1F0000;
  asm volatile("s_waitcnt lgkmcnt(0)" : "+s"(ka), "+s"(kb), "+v"(kvoff), "+s"(rot0));
  const rsrc_t rk = mk((const void*)ka[1], 32 * SK * DH * 2);
  kr[0] = ldnt(rk, kvoff, rot0);
  const unsigned long long t0 = __builtin_amdgcn_s_memrealtime();
  SB();
  int t0s = (int)((unsigned)t0 << 4);
  const bf16_t *x_ = (const bf16_t*)ka[0], *V_ = (const bf16_t*)ka[2], *wq_ = (const bf16_t*)ka[3], *wo_ = (const bf16_t*)ka[4], *w1_ = (const bf16_t*)ka[5], *b1_ = (const bf16_t*)ka[6],
               *w2_ = (const bf16_t*)ka[7], *b2_ = (const bf16_t*)kb[0], *gw_ = (const bf16_t*)kb[1], *out_ = (const bf16_t*)kb[2];
  unsigned char* ws = (unsigned char*)kb[3];
  const bool has_mlp = ii < 28;
  const int ug = xcd * 28 + (has_mlp ? ii : 0);
  const bool tq = ii < 16, th = !tq && ii < 23;
  const int tid1 = xcd * 7 + ii - 16, trb = tq ? (ii >> 1) & 3 : tid1 & 3, tstr = tq ? 8192 : F * 2;
  const int tcol = tq ? (2 * xcd + (ii >> 3)) * 512 + (ii & 1) * 256 : (tid1 >> 2) * 256;
  const bool gate_wg = ii == 28, b2_wg = bid == 29 * 8;
  const bool is_red = ii >= 25;
  const int red = is_red ? xcd * 7 + ii - 25 : 0;
  const int l16 = lane * 16;
  const bool w7 = w == 7;
  const int aoff = w7 ? (gate_wg ? (lane < 56 ? l16 : OOB) : (th && trb == 0 && lane < 16 ? tcol + l16 : OOB)) : (ii < 23 ? (112 * trb + 16 * w + 2 * r4) * tstr + tcol + c16 * 16 : OOB);
  const rsrc_t ra = mk(w7 ? (gate_wg ? x_ : b1_) : (tq ? wq_ : w1_), w7 ? (gate_wg ? 2 * D * 2 : F * 2) : D * tstr);
  const rsrc_t rv = mk(V_, 32 * SK * DH * 2);
  const rsrc_t rs = mk(ws, WS_END);
  int due = t0s, step = TAU, dend;

  f32x2 acc[4] = {{0.f, 0.f}, {0.f, 0.f}, {0.f, 0.f}, {0.f, 0.f}};
  float t0r[2], t1[2], t2[2], t3[2], t4[2], pa[8], pb[8];
  u32x4 ta0, ta1, ta2, ta3, ep, tg, qv, ml, qg, hg, wr[3]; u32x2 tsm; f32x4 pp, hsq;
  float sv, mw, mown; unsigned xa0, xa1, xb0, xb1, tch;
  LAS f32x4 *lo_w, *lo_r; LAS float *lr_w, *lf_r; LAS f32x2* lr_r;
  int lof, lww, lwr, ogoff, ygoff, outoff, yoff, ooff, qgoff, hgoff, mloff;

  static_for<71>([&](auto jc) __attribute__((always_inline)) {
    constexpr int j = decltype(jc)::value - 5;
    if constexpr (j == 11) SEG_ON(91); else if constexpr (j == 28) SEG_ON(92); else if constexpr (j == 46) SEG_END(93); else if constexpr (j == 63) SEG_ON(94);
    else if constexpr (padat(j)) PAD();
    if constexpr (j >= 1) asm volatile("s_waitcnt vmcnt(%0)" :: "i"(inflight(j)));
    if constexpr (j == 65) PIN(vr[31]);
    else if constexpr (j > -5) {
      const int late = pace(due);
      if constexpr (j >= 1 && j < JREB) { if (late > REBX) { due += late - REBX; t0s += late - REBX; } }
    }
    if constexpr (j == 63) dend = due;
    due += j == -5 ? TAU : j < 0 ? TAU * 41 / 64 : j == 0 ? TAU * 50 / 64 : j < 63 ? step : WFIRST * 16;
    SB();
    if constexpr (j == -5) {}
    else if constexpr (j == -4) ta0 = ldnt(ra, aoff, 0);
    else if constexpr (j == -3) ta1 = ldnt(ra, w7 && !gate_wg ? OOB : aoff, w7 ? D * 2 : tstr);
    else if constexpr (j == -2) ta2 = ldnt(w7 ? mk(gw_, gate_wg ? 896 : 0) : ra, aoff, w7 ? 0 : 8 * tstr);
    else if constexpr (j == -1) ta3 = ldnt(ra, w7 ? OOB : aoff, 9 * tstr);
    else if constexpr (j == 0) wr[2] = ldnt(mk((b2_wg ? b2_ : w2_ + (size_t)(ug * 8) * D) + w * 64, w7 ? 0 : (has_mlp ? 8 * 896 - w * 128 : (b2_wg ? 128 : 0))), r8 * 896 + ch * 16, 0);
    else if constexpr (j < 32) kr[j] = ldnt(rk, kvoff, (j * 65536 + rot) & 0x1F0000);
    else if constexpr (j < 64) vr[j - 32] = ldnt(rv, kvoff, (j * 65536 + rot) & 0x1F0000);
    else wr[j - 64] = ldnt(mk(wo_ + (size_t)(h * 256 + 16 * m16) * D + w * 64, w7 ? 0 : 16 * 896 - w * 128), r8 * 896 + ch * 16, (j - 64) * 8 * 896);
    SB();
    if constexpr (j == -5) {
      tch = 0;
      if (w7) tch = __builtin_nontemporal_load((const GAS unsigned*)(lane == 5 && is_red ? out_ : x_));
    }
    if constexpr (j == -4) ep = ld_sc1(rs, WS_EPOCH);
    if constexpr (j == JX) {
      const rsrc_t rx = mk(x_, w7 || ii >= 23 ? 0 : 2 * D * 2);
      xa0 = __builtin_amdgcn_raw_buffer_load_b32(rx, r4 * 4, (56 * trb + 8 * w) * 4, 0);
      xa1 = __builtin_amdgcn_raw_buffer_load_b32(rx, r4 * 4, (56 * trb + 8 * w) * 4 + D * 2, 0);
      xb0 = __builtin_amdgcn_raw_buffer_load_b32(rx, r4 * 4, (56 * trb + 8 * w + 4) * 4, 0);
      xb1 = __builtin_amdgcn_raw_buffer_load_b32(rx, r4 * 4, (56 * trb + 8 * w + 4) * 4 + D * 2, 0);
    }
    if constexpr (j == JA) PIN(tch);
    if constexpr (j == JREB) __builtin_amdgcn_raw_buffer_store_b64(u32x2{(unsigned)t0s, ep.x + 1u}, rs, (tid == 192 && ii % 3 == 1 && ii < 24) ? WS_T0 + (xcd * 8 + ii / 3) * 8 : OOB, 0, AUX_SC1);
    if constexpr (j == JG) { qg = ld_sc1(rs, w < 4 ? qgoff : OOB); tsm = __builtin_amdgcn_raw_buffer_load_b64(rs, w7 ? WS_T0 + lane * 8 : OOB, 0, AUX_SC1); }
    if constexpr (j == JH) hg = ld_sc1(rs, w7 ? hgoff : OOB);
    if constexpr (j == JML) ml = ld_sc1(rs, w7 ? mloff : OOB);
    SB();
    if constexpr (j == JA) {
      if (w7) { xa0 = 0x3F80u; xa1 = 0x3F80u; }
#pragma unroll
      for (int k = 0; k < 4; ++k) {
        const unsigned lo = perm_lo(ta0[k], ta1[k]), hi = perm_hi(ta0[k], ta1[k]), lo2 = perm_lo(ta2[k], ta3[k]), hi2 = perm_hi(ta2[k], ta3[k]);
        pa[2 * k] = dot2(lo2, xb0, dot2(lo, xa0, 0.f)); pb[2 * k] = dot2(lo2, xb1, dot2(lo, xa1, 0.f));
        pa[2 * k + 1] = dot2(hi2, xb0, dot2(hi, xa0, 0.f)); pb[2 * k + 1] = dot2(hi2, xb1, dot2(hi, xa1, 0.f));
        PIN(pa[2 * k]); PIN(pb[2 * k]); PIN(pa[2 * k + 1]); PIN(pb[2 * k + 1]);
      }
    }
    if constexpr (j == JA + 1) {
      float a[8];
#pragma unroll
      for (int k = 0; k < 8; ++k) a[k] = swap32_add(pa[k], pb[k]);
      f32x4 t;
#pragma unroll
      for (int k = 0; k < 4; ++k) t[k] = swap16_add(a[k], a[k + 4]);
      *(LAS f32x4*)(L + L_TP + w * 256 + half * 128 + c16 * 8 + (r4 & 1) * 4) = t;
    }
    if constexpr (j == JA + 2) {
      if (tid < 11 * CSL) L[L_C + tid] = 0.f;
      ep += 1u; tg = ep & TM;
      PIN(ep); PIN(tg);
      if (w7) {
        float ga = 0.f, gb = 0.f;
#pragma unroll
        for (int k = 0; k < 4; ++k) { ga = dot2(ta0[k], ta2[k], ga); gb = dot2(ta1[k], ta2[k], gb); }
        ga = 1.f / (1.f + __expf(-wave_sum(ga))); gb = 1.f / (1.f + __expf(-wave_sum(gb)));
        st_sc1(u32x4{asu(ga), ep.x, asu(gb), ep.y}, rs, gate_wg && lane == 0 ? WS_G + xcd * 128 : OOB);
      }
    }
    if constexpr (j == JB) {
      LDS_BAR();
      if (w == 0) {
        f32x4 t = *(LAS f32x4*)(L + L_TP + 4 * lane);
#pragma unroll
        for (int k = 1; k < 8; ++k) t += *(LAS f32x4*)(L + L_TP + k * 256 + 4 * lane);
        st_sc1(tagq(t, tg), rs, tq ? WS_QP + ((2 * xcd + (ii >> 3)) * 2 + half) * 4096 + trb * 1024 + (ii & 1) * 512 + c * 16 : (th ? WS_HP + tid1 * 1024 + l16 : OOB));
      }
    }
    if constexpr (j == 5) {
      qgoff = WS_QP + (h * 2 + b) * 4096 + (lane & 3) * 1024 + (w & 3) * 256 + (lane >> 2) * 16;
      hgoff = WS_HP + ((ug >> 4) * 4 + (lane & 3)) * 1024 + (r4 & 1) * 512 + (ug & 15) * 32 + half * 16;
      mloff = (lane & 8) ? WS_G + xcd * 128 : WS_ML + bh * 128 + ch * 16;
      PIN(qgoff); PIN(hgoff); PIN(mloff);
    }
    if constexpr (j == JB + 1) {
      lo_w = (LAS f32x4*)(L + L_OP + w * OPS + 8 * c + 4 * half);
      lo_r = (LAS f32x4*)(L + L_OP + ch * OPS + 32 * w + 4 * r8);
      lf_r = L + L_F + ch;
      lr_w = L + L_RED + ((lane & 12) == 0 ? w * 18 + (lane & 3) * 4 + half * 2 + (r4 & 1) : 160 + lane);
      lr_r = (LAS f32x2*)(L + L_RED + ch * 18 + 2 * r8);
      PIN(lo_w); PIN(lo_r); PIN(lf_r); PIN(lr_w); PIN(lr_r);
    }
    if constexpr (j == JB + 2) {
      lof = (int)(size_t)(L + L_C + (w7 ? (ch == 0 ? 7 * CSL + (r8 & 1) * 16 + r4 * 2 : 11 * CSL + lane * 2) : (7 - (c16 >> 1)) * CSL + (lane & 1) * 16 + r4 * 4));
      lww = (int)(size_t)(L + L_W + w * 1056) + r8 * 160 + ch * 16;
      lwr = (int)(size_t)(L + L_W + w * 1056) + (r4 < 2 ? r4 : 2) * 1408 + (c16 >> 2) * 160 + (lane & 3) * 8;
      PIN(lof); PIN(lww); PIN(lwr);
      if (!w7) *(LAS u32x4*)(size_t)(lww + 2 * 1408) = wr[2];
    }
    if constexpr (j == JB + 3) {
      ogoff = WS_O + (h * 16 + m16) * 1024 + c16 * 64 + r4 * 16;
      ygoff = WS_Y + red * 16384 + w * 2048 + l16;
      outoff = ch == 0 ? (red * 8 + r8) * 4 : OOB;
      PIN(ogoff); PIN(ygoff); PIN(outoff);
    }
    if constexpr (j == JB + 4) {
      yoff = (lane & 8) == 0 ? WS_Y + (((lane & 1) * 28 + 4 * w + (ch >> 1)) * 256 + xcd * 32 + ii) * 64 + r4 * 16 : OOB;
      ooff = ch == 0 ? WS_O + (h * 16 + 2 * w + (r8 >> 2)) * 1024 + m16 * 64 + (r8 & 3) * 16 : OOB;
      PIN(yoff); PIN(ooff);
    }
    if constexpr (j == JQ - 1) {
      PIN(tsm);
      if (w7) {
        const float d = wave_sum(tsm.y == ep.x ? (float)(int)(tsm.x - (unsigned)t0s) : 0.f) * (1.f / 64.f);
        if (lane == 0) ((LAS unsigned*)L)[L_T0] = (unsigned)t0s + (unsigned)(int)d;
      }
    }
    if constexpr (j == JQ) {
      PIN(qg);
      if (w < 4) {
        RETRY(qg, qgoff, clean(qg ^ tg, TMQ));
        qg &= ~TMQ;
        const u32x2 qq = {pk2(sum4(asf(qg.x)), sum4(asf(qg.y))), pk2(sum4(asf(qg.z)), sum4(asf(qg.w)))};
        if ((lane & 3) == 0) *(LAS u32x2*)(L + L_Q + w * 32 + (lane >> 2) * 2) = qq;
      }
    }
    if constexpr (j == JK0) {
      LDS_BAR();
      qv = *(LAS u32x4*)(L + L_Q + 4 * c);
      const float st = (float)(int)(((LAS unsigned*)L)[L_T0] - (unsigned)t0s) * (1.f / (62 - JK0));
      int sti = (int)__builtin_rintf(fminf(fmaxf(st, -100.f), 40.f)); PIN(sti);
      step = TAU + __builtin_amdgcn_readfirstlane(sti);
    }
    if constexpr (j == JH + LAG) {
      PIN(hg);
      if (w7) RETRY(hg, hgoff, clean(hg ^ tg, TMQ));
      hg &= ~TMQ;
#pragma unroll
      for (int k = 0; k < 4; ++k) {
        const float r = fmaxf(sum4(asf(hg[k])), 0.f);
        hsq[k] = b2_wg ? 1.f : r * r;
      }
      PIN(hsq);
    }
#pragma unroll
    for (int i = kdone(j - 1); i < kdone(j); ++i) {
      const u32x4 kv = kr[i];
      float d = dot2(kv.x, qv.x, 0.f); d = dot2(kv.y, qv.y, d); d = dot2(kv.z, qv.z, d); d = dot2(kv.w, qv.w, d);
      t0r[i & 1] = d;
      if ((i & 1) == 0) PIN(t0r[0]);
      if ((i & 1) == 1) { t1[(i >> 1) & 1] = swap16_add(t0r[0], t0r[1]); PIN(t1[(i >> 1) & 1]); }
      if ((i & 3) == 3) { const float a = t1[0] + dpp<XOR8>(t1[0]), bb = t1[1] + dpp<XOR8>(t1[1]); t2[(i >> 2) & 1] = (lane & 8) ? bb : a; PIN(t2[(i >> 2) & 1]); }
      if ((i & 7) == 7) { const float a = t2[0] + dpp<XOR7>(t2[0]), bb = t2[1] + dpp<XOR7>(t2[1]); t3[(i >> 3) & 1] = (lane & 4) ? bb : a; PIN(t3[(i >> 3) & 1]); }
      if ((i & 15) == 15) { const float a = t3[0] + dpp<XOR2>(t3[0]), bb = t3[1] + dpp<XOR2>(t3[1]); t4[(i >> 4) & 1] = (lane & 2) ? bb : a; PIN(t4[(i >> 4) & 1]); }
    }
    if constexpr (j == JS) {
      const float a = t4[0] + dpp<XOR1>(t4[0]), bb = t4[1] + dpp<XOR1>(t4[1]);
      sv = (lane & 1) ? bb : a;
      sv = bflo(pk2(sv, sv)) * (0.0625f * 1.4426950408889634f);
      mw = wave_max(sv);
      sv = __builtin_amdgcn_exp2f(sv - mw);
      L[L_P + w * 64 + half * 32 + (int)(__builtin_bitreverse32((unsigned)c) >> 27)] = sv;
      PIN(sv); PIN(mw);
    }
    if constexpr (j == JS + 1) {
      const float lw = wave_sum(sv);
      if (lane == 0) *(LAS f32x2*)(L + L_ML + 2 * w) = f32x2{mw, lw};
    }
    if constexpr (j == JS + 2) {
      LDS_BAR();
      const f32x2 t = *(LAS f32x2*)(L + L_ML + 2 * ch);
      mown = t[0];
      const float M = max8(mown);
      const float Lt = sum8(t[1] * __builtin_amdgcn_exp2f(mown - M));
      st_sc1(u32x4{asu(M), ep.x, asu(Lt), ep.y}, rs, tid == 448 ? WS_ML + bh * 128 + s * 16 : OOB);
      PIN(mown);
    }
    if constexpr (j == JML + LAG) {
      PIN(ml);
      if (w7) {
        RETRY(ml, mloff, ml.y == ep.x && ml.w == ep.y);
        const float g0 = dpp<XOR8>(asf(ml.x)), g1 = dpp<XOR8>(asf(ml.z));
        const f32x4 t = hsq * (1.f - ((lane & 16) ? g1 : g0));
        if (c16 == 0) *(LAS u32x2*)(L + L_C + 7 * CSL + (r4 & 1) * 16 + 8 + half * 2) = u32x2{pk2(t[0], t[1]), pk2(t[2], t[3])};
        const float mi = asf(ml.x), li = asf(ml.z);
        const float M = max8(mi);
        const float Lt = sum8(li * __builtin_amdgcn_exp2f(mi - M));
        const float f = (b ? g1 : g0) * __builtin_amdgcn_exp2f(mown - M) / Lt;
        if (lane < 8) *lf_r = f;
      }
    }
#pragma unroll
    for (int i = vdone(j - 1); i < vdone(j); ++i) {
      if ((i & 3) == 0) { pp = *(LAS f32x4*)(L + L_P + w * 64 + half * 32 + i); PIN(pp); }
      const u32x4 vv = vr[i];
      const f32x2 p2 = {pp[i & 3], pp[i & 3]};
#pragma unroll
      for (int k = 0; k < 4; ++k) { const f32x2 v2 = {bflo(vv[k]), bfhi(vv[k])}; acc[k] = __builtin_elementwise_fma(p2, v2, acc[k]); PIN(acc[k]); }
    }
    SB();
  });

  *lo_w = f32x4{swap32_add(acc[0][0], acc[2][0]), swap32_add(acc[0][1], acc[2][1]), swap32_add(acc[1][0], acc[3][0]), swap32_add(acc[1][1], acc[3][1])};
  LDS_BAR();
  st_sc1(tagv(sum8x4(*lo_r * *lf_r), tg), rs, ooff);
  SB();
  if (w7) {
    PAD();
    pace(dend + ODELAY * 16);
    u32x4 og;
#pragma nounroll
    for (int sp = 0; sp < SPIN_CAP; ++sp) {
      MEMB();
      og = ld_sc1(rs, ogoff) ^ tg;
      if (__all(clean(og, TM))) break;
    }
    const f32x4 t = sum8x4(__builtin_bit_cast(f32x4, og));
    *(LAS u32x2*)(size_t)lof = u32x2{pk2(t[0], t[1]), pk2(t[2], t[3])};
    LDS_BAR();
  } else {
    PAD();
    *(LAS u32x4*)(size_t)lww = wr[0];
    *(LAS u32x4*)(size_t)(lww + 1408) = wr[1];
    MEMB();
    u32x4 am[4], bm[4];
#pragma unroll
    for (int m = 0; m < 4; ++m) {
      const u32x2 lo = __builtin_bit_cast(u32x2, __builtin_amdgcn_ds_read_tr16_b64_v4i16((LAS s16x4*)(size_t)(lwr + m * 32)));
      const u32x2 hi = __builtin_bit_cast(u32x2, __builtin_amdgcn_ds_read_tr16_b64_v4i16((LAS s16x4*)(size_t)(lwr + m * 32 + 640)));
      am[m] = u32x4{lo.x, lo.y, hi.x, hi.y};
    }
    SB();
    LDS_BAR();
#pragma unroll
    for (int m = 0; m < 4; ++m) bm[m] = *(LAS u32x4*)(size_t)(lof + m * CSL * 4);
    SB();
    f32x4 t = {0.f, 0.f, 0.f, 0.f};
#pragma unroll
    for (int m = 0; m < 4; ++m) t = __builtin_amdgcn_mfma_f32_16x16x32_bf16(__builtin_bit_cast(bf16x8, am[m]), __builtin_bit_cast(bf16x8, bm[m]), t, 0, 0, 0);
    st_sc1(tagv(t, tg), rs, yoff);
  }
  if (is_red) {
    PAD();
    const int own = (int)((unsigned)__builtin_amdgcn_s_memrealtime() << 4) + (w7 ? YOWN + 10 : YOWN) * 16, plan = dend + YDELAY * 16;
    pace((int)((unsigned)own - (unsigned)plan) > 0 ? own : plan);
    u32x4 ya, yb;
#pragma nounroll
    for (int sp = 0; sp < SPIN_CAP; ++sp) {
      MEMB();
      ya = ld_sc1(rs, ygoff) ^ tg;
      yb = ld_sc1(rs, ygoff + 1024) ^ tg;
      if (__all(clean(ya | yb, TM))) break;
    }
    const f32x4 t = __builtin_bit_cast(f32x4, ya) + __builtin_bit_cast(f32x4, yb);
    float r = swap16_add(swap32_add(t[0], t[2]), swap32_add(t[1], t[3]));
    r += dpp<XOR8>(r); r += dpp<ROR4>(r);
    *lr_w = r;
    LDS_BAR();
    if (w == 0) {
      const f32x2 u = *lr_r;
      __builtin_amdgcn_raw_buffer_store_b32(pk2(sum8(u[0]), sum8(u[1])), mk(out_, 2 * D * 2), outoff, 0, 0);
    }
    if (red == 0 && w == 1) st_sc1(ep, rs, lane == 0 ? WS_EPOCH : OOB);
  }
  PAD_END();
}

extern "C" void kernel_launch(void* const* d_in, const int* in_sizes, int n_in, void* d_out, int out_size, void* d_ws, size_t ws_size, hipStream_t stream) {
  (void)hipFuncSetAttribute((const void*)fused, hipFuncAttributeMaxDynamicSharedMemorySize, LDS_BYTES);
  fused<<<256, NTHR, LDS_BYTES, stream>>>((const bf16_t*)d_in[0], (const bf16_t*)d_in[1], (const bf16_t*)d_in[2], (const bf16_t*)d_in[3], (const bf16_t*)d_in[4],
                                          (const bf16_t*)d_in[6], (const bf16_t*)d_in[7], (const bf16_t*)d_in[8], (const bf16_t*)d_in[9], (const bf16_t*)d_in[10],
                                          (bf16_t*)d_out, (unsigned char*)d_ws);
}
```
